```python
import torch

def _softmax(x, axis=-1):
    return torch.softmax(x, dim=axis)
def _gelu(x):
    return torch.nn.functional.gelu(x, approximate="tanh")
def _silu(x):
    return torch.nn.functional.silu(x)
def _sigmoid(x):
    return torch.sigmoid(x)
def _layernorm(x, axis=-1, eps=1e-5):
    m = x.mean(dim=axis, keepdim=True)
    v = x.var(dim=axis, keepdim=True, unbiased=False)
    return (x - m) / torch.sqrt(v + eps)
def _scatter_add(tgt, idx, src, axis=0):
    return tgt.clone().index_add_(axis, idx, src)
def _segment_sum(src, seg, n):
    out = torch.zeros((n,) + src.shape[1:], dtype=src.dtype)
    return out.index_add_(0, seg, src)
def _pad(x, pw):
    flat = tuple(v for pair in reversed(pw) for v in pair)
    return torch.nn.functional.pad(x, flat)

kernel_name = 'standalone__low_rank_proj_linformer__sampling'

def setup_inputs():

    _w = (lambda a: (lambda t: t.to(torch.bfloat16) if t.is_floating_point() else t)(torch.as_tensor(a)))
    B, H, Sq, Skv, Dh, R = 16, 32, 32, 4096, 128, 16
    return {
        "q": _w((lambda *s: torch.randn(*s, dtype=torch.bfloat16) * 0.8)(B, H, Sq, Dh)),
        "k": _w((lambda *s: torch.randn(*s, dtype=torch.bfloat16) * 0.8)(B, H, Skv, Dh)),
        "v": _w((lambda *s: torch.randn(*s, dtype=torch.bfloat16) * 0.05)(B, H, Skv, Dh)),
        "e_proj": _w((lambda *s: torch.randn(*s, dtype=torch.bfloat16) * 0.8)(R, Skv) * (Skv ** -0.5)),
        "f_proj": _w((lambda *s: torch.randn(*s, dtype=torch.bfloat16) * 0.05)(R, Skv) * (Skv ** -0.5)),
    }

def reference(q, k, v, e_proj, f_proj):
    B, H, Sq, Dh = q.shape
    kp = torch.einsum("rn,bhnd->bhrd", e_proj, k)
    vp = torch.einsum("rn,bhnd->bhrd", f_proj, v)
    scores = torch.einsum("bhqd,bhrd->bhqr", q, kp) * (Dh ** -0.5)
    p = _softmax(scores, axis=-1)
    o = torch.einsum("bhqr,bhrd->bhqd", p, vp)
    return o
```

```cpp
#include <hip/hip_runtime.h>
#include <hip/hip_bf16.h>
#include <stdint.h>
#include <stdio.h>

typedef unsigned u32x4 __attribute__((ext_vector_type(4)));
typedef unsigned u32x2 __attribute__((ext_vector_type(2)));
typedef float f32x4 __attribute__((ext_vector_type(4)));
typedef __bf16 bf16x8 __attribute__((ext_vector_type(8)));
#define GAS __attribute__((address_space(1)))

constexpr int NB = 16, NH = 32, SQ = 32, SKV = 4096, DH = 128, RK = 16;

#define NW 4
#define DEPTH 4
#define INTERLEAVE 0
#define USE_NT 1
#define WAVES_PER_EU 2

constexpr int NWK = NW / 2;
constexpr int NT = SKV / 32 / NWK;

__device__ __forceinline__ u32x4 ld_stream(const GAS u32x4* p) {
  return __builtin_nontemporal_load(p);
}
__device__ __forceinline__ f32x4 mfma32(u32x4 a, u32x4 b, f32x4 c) {
  return __builtin_amdgcn_mfma_f32_16x16x32_bf16(__builtin_bit_cast(bf16x8, a), __builtin_bit_cast(bf16x8, b), c, 0, 0, 0);
}
__device__ __forceinline__ unsigned pack_bf16(float lo, float hi) {
  typedef __bf16 bf16x2 __attribute__((ext_vector_type(2)));
  bf16x2 v; v.x = (__bf16)lo; v.y = (__bf16)hi;
  return __builtin_bit_cast(unsigned, v);
}
__device__ __forceinline__ float round_bf16(float x) {
  return (float)(__bf16)x;
}

struct Tile { u32x4 x[8]; u32x4 e; };

__device__ __forceinline__ int tile_row(int wk, int t) {
  return (wk * NT + t) * 32;
}

__device__ __forceinline__ void issue(Tile& tl, const GAS char* xb, const GAS char* eb, unsigned xo, unsigned eo, int row0) {
  const GAS char* xp = xb + (size_t)row0 * (DH * 2);
  const GAS char* ep = eb + (size_t)row0 * 2;
  tl.e = *(const GAS u32x4*)(ep + eo);
#pragma unroll
  for (int i = 0; i < 8; ++i) tl.x[i] = ld_stream((const GAS u32x4*)(xp + xo + i * (DH * 2)));
  __builtin_amdgcn_sched_barrier(0);
}

template <bool ISK>
__device__ __forceinline__ void consume(const Tile& tl, f32x4 (&acc)[8]) {
#pragma unroll
  for (int w = 0; w < 4; ++w) {
    u32x4 lo, hi;
#pragma unroll
    for (int p = 0; p < 4; ++p) {
      lo[p] = __builtin_amdgcn_perm(tl.x[2 * p + 1][w], tl.x[2 * p][w], 0x05040100u);
      hi[p] = __builtin_amdgcn_perm(tl.x[2 * p + 1][w], tl.x[2 * p][w], 0x07060302u);
    }
    if (ISK) {
      acc[2 * w] = mfma32(lo, tl.e, acc[2 * w]);
      acc[2 * w + 1] = mfma32(hi, tl.e, acc[2 * w + 1]);
    } else {
      acc[2 * w] = mfma32(tl.e, lo, acc[2 * w]);
      acc[2 * w + 1] = mfma32(tl.e, hi, acc[2 * w + 1]);
    }
  }
  __builtin_amdgcn_sched_barrier(0);
}

template <bool ISK>
__device__ __forceinline__ void stream(const GAS char* xb, const GAS char* eb, int wk, int lane, f32x4 (&acc)[8]) {
  const int c = lane & 15, g = lane >> 4;
  const unsigned xo = (unsigned)(g * 8 * (DH * 2) + c * 16);
  const unsigned eo = (unsigned)(c * (SKV * 2) + g * 16);
  Tile tl[DEPTH];
#pragma unroll
  for (int d = 0; d < DEPTH; ++d) issue(tl[d], xb, eb, xo, eo, tile_row(wk, d));
  constexpr int NMAIN = (NT - DEPTH) / DEPTH, REM = (NT - DEPTH) % DEPTH;
#pragma unroll 1
  for (int it = 0; it < NMAIN; ++it) {
#pragma unroll
    for (int d = 0; d < DEPTH; ++d) {
      consume<ISK>(tl[d], acc);
      issue(tl[d], xb, eb, xo, eo, tile_row(wk, it * DEPTH + d + DEPTH));
    }
  }
#pragma unroll
  for (int d = 0; d < REM; ++d) {
    consume<ISK>(tl[d], acc);
    issue(tl[d], xb, eb, xo, eo, tile_row(wk, NMAIN * DEPTH + d + DEPTH));
  }
#pragma unroll
  for (int k = 0; k < DEPTH; ++k) consume<ISK>(tl[(REM + k) % DEPTH], acc);
}

__global__ void __launch_bounds__(NW * 64, WAVES_PER_EU)
linformer_kernel(const __hip_bfloat16* __restrict__ q, const __hip_bfloat16* __restrict__ k,
                 const __hip_bfloat16* __restrict__ v, const __hip_bfloat16* __restrict__ e,
                 const __hip_bfloat16* __restrict__ f, __hip_bfloat16* __restrict__ out) {
  __shared__ __attribute__((aligned(16))) f32x4 red[NW * 8 * 64];
  const int bh = blockIdx.x;
  const int tid = threadIdx.x, lane = tid & 63;
  const int wave = __builtin_amdgcn_readfirstlane(tid >> 6);
  const int c = lane & 15, g = lane >> 4;
  f32x4 acc[8];
#pragma unroll
  for (int j = 0; j < 8; ++j) acc[j] = f32x4{0.f, 0.f, 0.f, 0.f};

  const size_t head_off = (size_t)bh * SKV * DH * 2;
  if (wave < NWK) stream<true>((const GAS char*)k + head_off, (const GAS char*)e, wave, lane, acc);
  else stream<false>((const GAS char*)v + head_off, (const GAS char*)f, wave - NWK, lane, acc);

  const int u = wave & 1;
  u32x4 qf[4];
  {
    const GAS char* qp = (const GAS char*)q + ((size_t)bh * SQ + 16 * u + c) * (DH * 2) + g * 64;
#pragma unroll
    for (int t = 0; t < 4; ++t) qf[t] = *(const GAS u32x4*)(qp + t * 16);
  }
#pragma unroll
  for (int j = 0; j < 8; ++j) red[(wave * 8 + j) * 64 + lane] = acc[j];
  __syncthreads();
  if (wave >= 2) return;

  f32x4 kp[8], vp[8];
#pragma unroll
  for (int j = 0; j < 8; ++j) {
    kp[j] = red[j * 64 + lane];
    vp[j] = red[(NWK * 8 + j) * 64 + lane];
#pragma unroll
    for (int w = 1; w < NWK; ++w) {
      kp[j] += red[(w * 8 + j) * 64 + lane];
      vp[j] += red[((NWK + w) * 8 + j) * 64 + lane];
    }
  }
  f32x4 s = f32x4{0.f, 0.f, 0.f, 0.f};
#pragma unroll
  for (int t = 0; t < 4; ++t) {
    u32x4 a;
#pragma unroll
    for (int m = 0; m < 4; ++m) a[m] = pack_bf16(kp[2 * m][t], kp[2 * m + 1][t]);
    s = mfma32(a, qf[t], s);
  }
  float sc[4];
#pragma unroll
  for (int t = 0; t < 4; ++t) sc[t] = round_bf16(round_bf16(s[t]) * 0.08838834764831845f);
  float mx = fmaxf(fmaxf(sc[0], sc[1]), fmaxf(sc[2], sc[3]));
  mx = fmaxf(mx, __shfl_xor(mx, 16));
  mx = fmaxf(mx, __shfl_xor(mx, 32));
  float sum = 0.f;
#pragma unroll
  for (int t = 0; t < 4; ++t) { sc[t] = __expf(sc[t] - mx); sum += sc[t]; }
  sum += __shfl_xor(sum, 16);
  sum += __shfl_xor(sum, 32);
  const float inv = 1.0f / sum;
  u32x4 pa;
  pa[0] = pack_bf16(sc[0] * inv, sc[1] * inv);
  pa[1] = pack_bf16(sc[2] * inv, sc[3] * inv);
  pa[2] = 0u; pa[3] = 0u;
  f32x4 o[8];
#pragma unroll
  for (int j = 0; j < 8; ++j) {
    u32x4 b;
    b[0] = pack_bf16(vp[j][0], vp[j][1]);
    b[1] = pack_bf16(vp[j][2], vp[j][3]);
    b[2] = 0u; b[3] = 0u;
    o[j] = mfma32(pa, b, f32x4{0.f, 0.f, 0.f, 0.f});
  }
  GAS char* op = (GAS char*)out + ((size_t)bh * SQ + 16 * u + 4 * g) * (DH * 2) + c * 16;
#pragma unroll
  for (int t = 0; t < 4; ++t) {
    u32x4 w;
#pragma unroll
    for (int m = 0; m < 4; ++m) w[m] = pack_bf16(o[2 * m][t], o[2 * m + 1][t]);
    *(GAS u32x4*)(op + t * (DH * 2)) = w;
  }
}

extern "C" void kernel_launch(void* const* d_in, const int* in_sizes, int n_in, void* d_out, int out_size,
                              void* d_ws, size_t ws_size, hipStream_t stream) {
  fprintf(stderr, "PTRS q=%p k=%p v=%p e=%p f=%p out=%p ws=%p ws_size=%zu\n", d_in[0], d_in[1], d_in[2], d_in[3], d_in[4], d_out, d_ws, ws_size);
  const __hip_bfloat16* q = (const __hip_bfloat16*)d_in[0];
  const __hip_bfloat16* k = (const __hip_bfloat16*)d_in[1];
  const __hip_bfloat16* v = (const __hip_bfloat16*)d_in[2];
  const __hip_bfloat16* e = (const __hip_bfloat16*)d_in[3];
  const __hip_bfloat16* f = (const __hip_bfloat16*)d_in[4];
  linformer_kernel<<<NB * NH, NW * 64, 0, stream>>>(q, k, v, e, f, (__hip_bfloat16*)d_out);
}
```

```cpp
#include <hip/hip_runtime.h>
#include <hip/hip_bf16.h>
#include <stdint.h>

typedef unsigned u32x4 __attribute__((ext_vector_type(4)));
typedef unsigned u32x2 __attribute__((ext_vector_type(2)));
typedef float f32x4 __attribute__((ext_vector_type(4)));
typedef __bf16 bf16x8 __attribute__((ext_vector_type(8)));
#define GAS __attribute__((address_space(1)))

constexpr int NB = 16, NH = 32, SQ = 32, SKV = 4096, DH = 128, RK = 16;

#define DEPTH 4
#define USE_NT 1
constexpr int NW = 4;
constexpr int HPW = 2;
constexpr int TPS = SKV / 32 / NW;
constexpr int NSEQ = TPS * 2 * HPW;
static_assert(TPS % DEPTH == 0, "depth must divide the tiles of a stream");

__device__ __forceinline__ u32x4 ld_stream(const GAS u32x4* p) {
  return __builtin_nontemporal_load(p);
}
__device__ __forceinline__ f32x4 mfma32(u32x4 a, u32x4 b, f32x4 c) {
  return __builtin_amdgcn_mfma_f32_16x16x32_bf16(__builtin_bit_cast(bf16x8, a), __builtin_bit_cast(bf16x8, b), c, 0, 0, 0);
}
__device__ __forceinline__ unsigned pack_bf16(float lo, float hi) {
  typedef __bf16 bf16x2 __attribute__((ext_vector_type(2)));
  bf16x2 v; v.x = (__bf16)lo; v.y = (__bf16)hi;
  return __builtin_bit_cast(unsigned, v);
}
__device__ __forceinline__ float round_bf16(float x) { return (float)(__bf16)x; }

struct Tile { u32x4 x[8]; u32x4 e; };

struct Ctx {
  const GAS char* k; const GAS char* v; const GAS char* e; const GAS char* f;
  unsigned xo, eo;
  int wave;
};

__device__ __forceinline__ void issue(Tile& tl, const Ctx& c, int s) {
  const int phase = s / TPS, t = (s % TPS) * NW + c.wave;
  const GAS char* xp = ((phase & 1) ? c.v : c.k) + (size_t)(phase >> 1) * (SKV * DH * 2) + (size_t)t * 8192;
  const GAS char* ep = ((phase & 1) ? c.f : c.e) + (size_t)t * 64;
  tl.e = *(const GAS u32x4*)(ep + c.eo);
#pragma unroll
  for (int i = 0; i < 8; ++i) tl.x[i] = ld_stream((const GAS u32x4*)(xp + c.xo + i * (DH * 2)));
  __builtin_amdgcn_sched_barrier(0);
}

template <bool ISK>
__device__ __forceinline__ void consume(const Tile& tl, f32x4 (&acc)[8]) {
#pragma unroll
  for (int w = 0; w < 4; ++w) {
    u32x4 lo, hi;
#pragma unroll
    for (int p = 0; p < 4; ++p) {
      lo[p] = __builtin_amdgcn_perm(tl.x[2 * p + 1][w], tl.x[2 * p][w], 0x05040100u);
      hi[p] = __builtin_amdgcn_perm(tl.x[2 * p + 1][w], tl.x[2 * p][w], 0x07060302u);
    }
    if (ISK) {
      acc[2 * w] = mfma32(lo, tl.e, acc[2 * w]);
      acc[2 * w + 1] = mfma32(hi, tl.e, acc[2 * w + 1]);
    } else {
      acc[2 * w] = mfma32(tl.e, lo, acc[2 * w]);
      acc[2 * w + 1] = mfma32(tl.e, hi, acc[2 * w + 1]);
    }
  }
  __builtin_amdgcn_sched_barrier(0);
}

template <bool ISK, bool LAST>
__device__ __forceinline__ void phase_run(Tile (&tl)[DEPTH], const Ctx& c, int s0, f32x4 (&acc)[8]) {
#pragma unroll
  for (int j = 0; j < 8; ++j) acc[j] = f32x4{0.f, 0.f, 0.f, 0.f};
  constexpr int NIT = TPS / DEPTH - (LAST ? 1 : 0);
#pragma unroll 1
  for (int it = 0; it < NIT; ++it) {
#pragma unroll
    for (int d = 0; d < DEPTH; ++d) {
      consume<ISK>(tl[d], acc);
      issue(tl[d], c, s0 + it * DEPTH + d + DEPTH);
    }
  }
  if (LAST) {
#pragma unroll
    for (int d = 0; d < DEPTH; ++d) consume<ISK>(tl[d], acc);
  }
}

constexpr int LDS_Q = 0, LDS_RED = 16384, LDS_BYTES = LDS_RED + 2 * 2 * NW * 8192;

__device__ __forceinline__ void epilogue(const unsigned char* smem, int hsel, int wave, int lane, GAS char* out_head) {
  const int c = lane & 15, g = lane >> 4;
  const int u = wave & 1, half = wave >> 1;
  const f32x4* redk = (const f32x4*)(smem + LDS_RED + (hsel * 2 + 0) * NW * 8192);
  const f32x4* redv = (const f32x4*)(smem + LDS_RED + (hsel * 2 + 1) * NW * 8192);
  f32x4 kp[8], vp[4];
#pragma unroll
  for (int j = 0; j < 8; ++j) {
    kp[j] = redk[j * 64 + lane];
#pragma unroll
    for (int w = 1; w < NW; ++w) kp[j] += redk[(w * 8 + j) * 64 + lane];
  }
#pragma unroll
  for (int j = 0; j < 4; ++j) {
    vp[j] = redv[(half * 4 + j) * 64 + lane];
#pragma unroll
    for (int w = 1; w < NW; ++w) vp[j] += redv[(w * 8 + half * 4 + j) * 64 + lane];
  }
  const unsigned char* qp = smem + LDS_Q + hsel * 8192 + (16 * u + c) * 256 + g * 64;
  f32x4 s = f32x4{0.f, 0.f, 0.f, 0.f};
#pragma unroll
  for (int t = 0; t < 4; ++t) {
    u32x4 a;
#pragma unroll
    for (int m = 0; m < 4; ++m) a[m] = pack_bf16(kp[2 * m][t], kp[2 * m + 1][t]);
    const u32x4 qf = *(const u32x4*)(qp + ((t * 16) ^ ((c & 3) << 4)));
    s = mfma32(a, qf, s);
  }
  float sc[4];
#pragma unroll
  for (int t = 0; t < 4; ++t) sc[t] = round_bf16(round_bf16(s[t]) * 0.08838834764831845f);
  float mx = fmaxf(fmaxf(sc[0], sc[1]), fmaxf(sc[2], sc[3]));
  mx = fmaxf(mx, __shfl_xor(mx, 16));
  mx = fmaxf(mx, __shfl_xor(mx, 32));
  float sum = 0.f;
#pragma unroll
  for (int t = 0; t < 4; ++t) { sc[t] = __expf(sc[t] - mx); sum += sc[t]; }
  sum += __shfl_xor(sum, 16);
  sum += __shfl_xor(sum, 32);
  const float inv = 1.0f / sum;
  u32x4 pa;
  pa[0] = pack_bf16(sc[0] * inv, sc[1] * inv);
  pa[1] = pack_bf16(sc[2] * inv, sc[3] * inv);
  pa[2] = 0u; pa[3] = 0u;
  f32x4 o[4];
#pragma unroll
  for (int j = 0; j < 4; ++j) {
    u32x4 b;
    b[0] = pack_bf16(vp[j][0], vp[j][1]);
    b[1] = pack_bf16(vp[j][2], vp[j][3]);
    b[2] = 0u; b[3] = 0u;
    o[j] = mfma32(pa, b, f32x4{0.f, 0.f, 0.f, 0.f});
  }
  GAS char* op = out_head + (16 * u + 4 * g) * (DH * 2) + c * 16 + half * 8;
#pragma unroll
  for (int t = 0; t < 4; ++t) {
    u32x2 w;
    w[0] = pack_bf16(o[0][t], o[1][t]);
    w[1] = pack_bf16(o[2][t], o[3][t]);
    *(GAS u32x2*)(op + t * (DH * 2)) = w;
  }
}

__global__ void __launch_bounds__(NW * 64, 1)
linformer_kernel(const __hip_bfloat16* __restrict__ q, const __hip_bfloat16* __restrict__ k,
                 const __hip_bfloat16* __restrict__ v, const __hip_bfloat16* __restrict__ e,
                 const __hip_bfloat16* __restrict__ f, __hip_bfloat16* __restrict__ out) {
  extern __shared__ __attribute__((aligned(16))) unsigned char smem[];
  const int tid = threadIdx.x, lane = tid & 63;
  const int wave = __builtin_amdgcn_readfirstlane(tid >> 6);
  const int c = lane & 15, g = lane >> 4;
  const int bh0 = blockIdx.x * HPW;
  Ctx cx;
  cx.k = (const GAS char*)k + (size_t)bh0 * (SKV * DH * 2);
  cx.v = (const GAS char*)v + (size_t)bh0 * (SKV * DH * 2);
  cx.e = (const GAS char*)e;
  cx.f = (const GAS char*)f;
  cx.xo = (unsigned)(g * 8 * (DH * 2) + c * 16);
  cx.eo = (unsigned)(c * (SKV * 2) + g * 16);
  cx.wave = wave;

  u32x4 qreg[4];
  const GAS char* qg = (const GAS char*)q + (size_t)bh0 * (SQ * DH * 2);
#pragma unroll
  for (int i = 0; i < 4; ++i) qreg[i] = *(const GAS u32x4*)(qg + (i * 256 + tid) * 16);
  __builtin_amdgcn_sched_barrier(0);

  Tile tl[DEPTH];
#pragma unroll
  for (int d = 0; d < DEPTH; ++d) issue(tl[d], cx, d);

#pragma unroll
  for (int i = 0; i < 4; ++i) {
    const int idx = i * 256 + tid, row = idx >> 4, ch = idx & 15;
    *(u32x4*)(smem + LDS_Q + row * 256 + ((ch ^ (row & 3)) << 4)) = qreg[i];
  }
  __builtin_amdgcn_sched_barrier(0);

  f32x4 acc[8];
  f32x4* red = (f32x4*)(smem + LDS_RED);
  phase_run<true, false>(tl, cx, 0 * TPS, acc);
#pragma unroll
  for (int j = 0; j < 8; ++j) red[((0 * NW + wave) * 8 + j) * 64 + lane] = acc[j];
  phase_run<false, false>(tl, cx, 1 * TPS, acc);
#pragma unroll
  for (int j = 0; j < 8; ++j) red[((1 * NW + wave) * 8 + j) * 64 + lane] = acc[j];
  __syncthreads();
  epilogue(smem, 0, wave, lane, (GAS char*)out + (size_t)bh0 * (SQ * DH * 2));
  phase_run<true, false>(tl, cx, 2 * TPS, acc);
#pragma unroll
  for (int j = 0; j < 8; ++j) red[((2 * NW + wave) * 8 + j) * 64 + lane] = acc[j];
  phase_run<false, true>(tl, cx, 3 * TPS, acc);
#pragma unroll
  for (int j = 0; j < 8; ++j) red[((3 * NW + wave) * 8 + j) * 64 + lane] = acc[j];
  __syncthreads();
  epilogue(smem, 1, wave, lane, (GAS char*)out + (size_t)(bh0 + 1) * (SQ * DH * 2));
}

extern "C" void kernel_launch(void* const* d_in, const int* in_sizes, int n_in, void* d_out, int out_size,
                              void* d_ws, size_t ws_size, hipStream_t stream) {
  const __hip_bfloat16* q = (const __hip_bfloat16*)d_in[0];
  const __hip_bfloat16* k = (const __hip_bfloat16*)d_in[1];
  const __hip_bfloat16* v = (const __hip_bfloat16*)d_in[2];
  const __hip_bfloat16* e = (const __hip_bfloat16*)d_in[3];
  const __hip_bfloat16* f = (const __hip_bfloat16*)d_in[4];
  (void)hipFuncSetAttribute((const void*)linformer_kernel, hipFuncAttributeMaxDynamicSharedMemorySize, LDS_BYTES);
  linformer_kernel<<<NB * NH / HPW, NW * 64, LDS_BYTES, stream>>>(q, k, v, e, f, (__hip_bfloat16*)d_out);
}
```

```cpp
#include <hip/hip_runtime.h>
#include <hip/hip_bf16.h>
#include <stdint.h>

typedef unsigned u32x4 __attribute__((ext_vector_type(4)));
typedef unsigned u32x2 __attribute__((ext_vector_type(2)));
typedef float f32x4 __attribute__((ext_vector_type(4)));
typedef __bf16 bf16x8 __attribute__((ext_vector_type(8)));
#define GAS __attribute__((address_space(1)))
#define LAS __attribute__((address_space(3)))

constexpr int NB = 16, NH = 32, SQ = 32, SKV = 4096, DH = 128;
constexpr int NW = 4;
constexpr int HPW = 2;
constexpr int TPS = SKV / 32 / NW;
constexpr int NSEQ = TPS * 2 * HPW;
constexpr int HEAD_BYTES = SKV * DH * 2;
constexpr int AUX_NT = 2, AUX_SC1 = 16;

constexpr int NFLY = 8, WIN_MID = 5, WIN_AHEAD = 4;
constexpr unsigned PERIOD4 = 868;
constexpr unsigned T_START = 280;
constexpr unsigned T_LAG = 75;
constexpr int SPRINT = 2;

__device__ __forceinline__ f32x4 mfma32(u32x4 a, u32x4 b, f32x4 c) {
  return __builtin_amdgcn_mfma_f32_16x16x32_bf16(__builtin_bit_cast(bf16x8, a), __builtin_bit_cast(bf16x8, b), c, 0, 0, 0);
}
__device__ __forceinline__ unsigned pack_bf16(float lo, float hi) {
  typedef __bf16 bf16x2 __attribute__((ext_vector_type(2)));
  bf16x2 v; v.x = (__bf16)lo; v.y = (__bf16)hi;
  return __builtin_bit_cast(unsigned, v);
}
__device__ __forceinline__ float round_bf16(float x) { return (float)(__bf16)x; }

struct Ctx {
  const char* k; const char* v; const char* e; const char* f;
  unsigned xo, eo;
  int tw;
};
struct Srd { __amdgpu_buffer_rsrc_t x, e; };

__device__ __forceinline__ Srd tile_srd(const Ctx& c, int s) {
  const int phase = (s / TPS) & 3, t = ((s % TPS) * NW + c.tw) & (SKV / 32 - 1);
  const bool valid = s < NSEQ;
  const char* xp = ((phase & 2) ? c.v : c.k) + (size_t)(phase & 1) * HEAD_BYTES + (size_t)t * 8192;
  const char* ep = ((phase & 2) ? c.f : c.e) + (size_t)t * 64;
  Srd r;
  r.x = __builtin_amdgcn_make_buffer_rsrc((void*)xp, 0, valid ? 8192 : 0, 0x00020000);
  r.e = __builtin_amdgcn_make_buffer_rsrc((void*)ep, 0, valid ? 15 * SKV * 2 + 64 : 0, 0x00020000);
  return r;
}
__device__ __forceinline__ u32x4 ld_x(const Srd& r, const Ctx& c, int row) {
  return __builtin_amdgcn_raw_buffer_load_b128(r.x, c.xo + row * (DH * 2), 0, AUX_NT);
}
__device__ __forceinline__ u32x4 ld_e(const Srd& r, const Ctx& c) { return __builtin_amdgcn_raw_buffer_load_b128(r.e, c.eo, 0, 0); }

constexpr int PART_W = 8192, PART_BUF = NW * PART_W;
constexpr int LDS_PART = 0, LDS_Q = 4 * PART_BUF, LDS_KB = LDS_Q + 16384, LDS_P1 = LDS_KB + 4096, LDS_CNT = LDS_P1 + 1024;
constexpr int LDS_BYTES = LDS_CNT + 16;

__device__ __forceinline__ f32x4 sum_part(const unsigned char* part, int j, int slot) {
  const unsigned char* p = part + j * 1024 + slot * 16;
  f32x4 x = *(const f32x4*)p;
#pragma unroll
  for (int w = 1; w < NW; ++w) x += *(const f32x4*)(p + w * PART_W);
  return x;
}

__device__ __forceinline__ u32x2 softmax_p(const unsigned char* smem, int hh, int u, int lane) {
  const int c = lane & 15, g = lane >> 4;
  const unsigned char* qp = smem + LDS_Q + hh * 8192 + (16 * u + c) * 256;
  const unsigned char* kp = smem + LDS_KB + c * 256;
  f32x4 s = f32x4{0.f, 0.f, 0.f, 0.f};
#pragma unroll
  for (int st = 0; st < 4; ++st) {
    const int pos = ((4 * st + g) ^ c) << 4;
    s = mfma32(*(const u32x4*)(kp + pos), *(const u32x4*)(qp + pos), s);
  }
  float sc[4];
#pragma unroll
  for (int t = 0; t < 4; ++t) sc[t] = round_bf16(round_bf16(s[t]) * 0.08838834764831845f);
  float mx = fmaxf(fmaxf(sc[0], sc[1]), fmaxf(sc[2], sc[3]));
  mx = fmaxf(mx, __shfl_xor(mx, 16));
  mx = fmaxf(mx, __shfl_xor(mx, 32));
  float sum = 0.f;
#pragma unroll
  for (int t = 0; t < 4; ++t) { sc[t] = __expf(sc[t] - mx); sum += sc[t]; }
  sum += __shfl_xor(sum, 16);
  sum += __shfl_xor(sum, 32);
  const float inv = 1.0f / sum;
  return u32x2{pack_bf16(sc[0] * inv, sc[1] * inv), pack_bf16(sc[2] * inv, sc[3] * inv)};
}

__device__ __forceinline__ void wait_partials(const unsigned char* smem, int phase) {
  volatile LAS unsigned* cnt = (volatile LAS unsigned*)(LAS unsigned char*)(smem + LDS_CNT) + phase;
  while (*cnt < (unsigned)NW) __builtin_amdgcn_s_sleep(16);
  __builtin_amdgcn_fence(__ATOMIC_ACQUIRE, "workgroup");
}

__device__ __forceinline__ __amdgpu_buffer_rsrc_t out_srd(__hip_bfloat16* out, int bh0) {
  return __builtin_amdgcn_make_buffer_rsrc((char*)out + (size_t)bh0 * (SQ * DH * 2), 0, HPW * SQ * DH * 2, 0x00020000);
}

__global__ void __launch_bounds__((NW + 1) * 64, 2)
linformer_kernel(const __hip_bfloat16* __restrict__ q, const __hip_bfloat16* __restrict__ k,
                 const __hip_bfloat16* __restrict__ v, const __hip_bfloat16* __restrict__ e,
                 const __hip_bfloat16* __restrict__ f, __hip_bfloat16* __restrict__ out) {
  extern __shared__ __attribute__((aligned(16))) unsigned char smem[];
  const int tid = threadIdx.x, lane = tid & 63;
  const int wave = __builtin_amdgcn_readfirstlane(tid >> 6);
  const int c = lane & 15, g = lane >> 4;
  const int bh0 = blockIdx.x * HPW;

  if (wave == NW) {
    if (lane < 4) *((LAS unsigned*)(LAS unsigned char*)(smem + LDS_CNT) + lane) = 0u;
    __syncthreads();
    {
      const GAS char* qg = (const GAS char*)q + (size_t)bh0 * (SQ * DH * 2);
      u32x4 qreg[16];
#pragma unroll
      for (int i = 0; i < 16; ++i) qreg[i] = __builtin_nontemporal_load((const GAS u32x4*)(qg + (i * 64 + lane) * 16));
#pragma unroll
      for (int i = 0; i < 16; ++i) {
        const int row = i * 4 + g;
        *(u32x4*)(smem + LDS_Q + row * 256 + ((c ^ (row & 15)) << 4)) = qreg[i];
      }
    }
    u32x2 p0[2];
#pragma unroll 1
    for (int hh = 0; hh < 2; ++hh) {
      wait_partials(smem, hh);
      f32x4 x[8];
#pragma unroll
      for (int j = 0; j < 8; ++j) x[j] = sum_part(smem + LDS_PART + hh * PART_BUF, j, lane ^ ((j >> 2) << 3));
#pragma unroll
      for (int t = 0; t < 4; ++t) {
        const int r = 4 * g + t;
        *(u32x4*)(smem + LDS_KB + r * 256 + ((c ^ r) << 4)) =
            u32x4{pack_bf16(x[0][t], x[1][t]), pack_bf16(x[2][t], x[3][t]), pack_bf16(x[4][t], x[5][t]), pack_bf16(x[6][t], x[7][t])};
      }
      const u32x2 pa = softmax_p(smem, hh, 0, lane), pb = softmax_p(smem, hh, 1, lane);
      if (hh == 0) { p0[0] = pa; p0[1] = pb; }
      else { *(u32x2*)(smem + LDS_P1 + lane * 8) = pa; *(u32x2*)(smem + LDS_P1 + 512 + lane * 8) = pb; }
    }
    wait_partials(smem, 2);
    u32x4 o0[2][4];
    {
      u32x2 b[8];
#pragma unroll
      for (int j = 0; j < 8; ++j) {
        const f32x4 x = sum_part(smem + LDS_PART + 2 * PART_BUF, j, lane ^ ((j >> 2) << 3));
        b[j] = u32x2{pack_bf16(x[0], x[1]), pack_bf16(x[2], x[3])};
      }
#pragma unroll
      for (int u = 0; u < 2; ++u) {
        f32x4 o[8];
#pragma unroll
        for (int j = 0; j < 8; ++j)
          o[j] = mfma32(u32x4{p0[u][0], p0[u][1], 0u, 0u}, u32x4{b[j][0], b[j][1], 0u, 0u}, f32x4{0.f, 0.f, 0.f, 0.f});
#pragma unroll
        for (int t = 0; t < 4; ++t)
          o0[u][t] = u32x4{pack_bf16(o[0][t], o[1][t]), pack_bf16(o[2][t], o[3][t]), pack_bf16(o[4][t], o[5][t]), pack_bf16(o[6][t], o[7][t])};
      }
    }
    __syncthreads();
    const __amdgpu_buffer_rsrc_t ro = out_srd(out, bh0);
#pragma unroll
    for (int u = 0; u < 2; ++u)
#pragma unroll
      for (int t = 0; t < 4; ++t)
        __builtin_amdgcn_raw_buffer_store_b128(o0[u][t], ro, (16 * u + 4 * g + t) * (DH * 2) + c * 16, 0, AUX_SC1);
    return;
  }

  const unsigned sched_t0 = (unsigned)__builtin_amdgcn_s_memrealtime();
  Ctx cx;
  cx.k = (const char*)k + (size_t)bh0 * HEAD_BYTES;
  cx.v = (const char*)v + (size_t)bh0 * HEAD_BYTES;
  cx.e = (const char*)e;
  cx.f = (const char*)f;
  cx.xo = (unsigned)(g * 8 * (DH * 2) + c * 16);
  cx.eo = (unsigned)(c * (SKV * 2) + g * 16);
  cx.tw = wave + (blockIdx.x >> 3) * NW;

  u32x4 R[16], ef[2];
  Srd cur = tile_srd(cx, 0);
  ef[0] = ld_e(cur, cx);
#pragma unroll
  for (int L = 0; L < NFLY; ++L) R[L] = ld_x(cur, cx, L);
  static_assert(NFLY == 8, "the ring below requests tile s + 1 while it consumes tile s");
  __builtin_amdgcn_sched_barrier(0);
  __syncthreads();

#pragma unroll 1
  for (int phase = 0; phase < 2 * HPW; ++phase) {
    f32x4 acc[8];
#pragma unroll
    for (int j = 0; j < 8; ++j) acc[j] = f32x4{0.f, 0.f, 0.f, 0.f};
#pragma unroll 1
    for (int it = 0; it < TPS / 2; ++it) {
      const int s0 = phase * TPS + it * 2;
      const unsigned el = (unsigned)__builtin_amdgcn_s_memrealtime() - sched_t0, tg = (unsigned)(s0 >> 1) * PERIOD4 / 4 + T_START;
      const unsigned lvl = __builtin_amdgcn_readfirstlane((s0 >> 1) >= NSEQ / 2 - SPRINT ? 0u : (el < tg ? 2u : (el < tg + T_LAG ? 1u : 0u)));
      u32x4 lo[4], hi[4];
#pragma unroll
      for (int st = 0; st < 8; ++st) {
        if ((st & 3) == 0) cur = tile_srd(cx, s0 + (st >> 2) + 1);
        __builtin_amdgcn_sched_barrier(0);
#pragma unroll
        for (int w = 0; w < 4; ++w) {
          lo[w][st & 3] = __builtin_amdgcn_perm(R[2 * st + 1][w], R[2 * st][w], 0x05040100u);
          hi[w][st & 3] = __builtin_amdgcn_perm(R[2 * st + 1][w], R[2 * st][w], 0x07060302u);
        }
        __builtin_amdgcn_sched_barrier(0);
        asm volatile("s_cmp_eq_u32 %0, 0\n\ts_cbranch_scc1 .Lthr%=\n\ts_waitcnt vmcnt(%1)\n\ts_cmp_eq_u32 %0, 1\n\t"
                     "s_cbranch_scc1 .Lthr%=\n\ts_waitcnt vmcnt(%2)\n.Lthr%=:" ::"s"(lvl), "n"(WIN_MID), "n"(WIN_AHEAD) : "scc");
        if ((st & 3) == 0) ef[((st >> 2) + 1) & 1] = ld_e(cur, cx);
        R[(2 * st + NFLY) % 16] = ld_x(cur, cx, (2 * st) & 7);
        R[(2 * st + NFLY + 1) % 16] = ld_x(cur, cx, (2 * st + 1) & 7);
        __builtin_amdgcn_sched_barrier(0);
        if ((st & 3) == 3) {
#pragma unroll
          for (int w = 0; w < 4; ++w) {
            acc[2 * w] = mfma32(ef[st >> 2], lo[w], acc[2 * w]);
            acc[2 * w + 1] = mfma32(ef[st >> 2], hi[w], acc[2 * w + 1]);
          }
          __builtin_amdgcn_sched_barrier(0);
        }
      }
    }
    unsigned char* part = smem + LDS_PART + phase * PART_BUF + wave * PART_W;
#pragma unroll
    for (int j = 0; j < 8; ++j) *(f32x4*)(part + j * 1024 + (lane ^ ((j >> 2) << 3)) * 16) = acc[j];
    if (phase < 3) {
      __builtin_amdgcn_fence(__ATOMIC_RELEASE, "workgroup");
      if (lane == 0)
        __hip_atomic_fetch_add((LAS unsigned*)(LAS unsigned char*)(smem + LDS_CNT) + phase, 1u, __ATOMIC_RELAXED, __HIP_MEMORY_SCOPE_WORKGROUP);
    }
  }
  __syncthreads();
  {
    const int u = wave & 1, h = wave >> 1;
    const int fslot = (8 * h + (c >> 1) + 16 * g) ^ ((c & 1) << 3), fj = 4 * (c & 1);
    const unsigned char* part = smem + LDS_PART + 3 * PART_BUF;
    const u32x2 p1 = *(const u32x2*)(smem + LDS_P1 + u * 512 + lane * 8);
    f32x4 o[4];
#pragma unroll
    for (int jj = 0; jj < 4; ++jj) {
      const f32x4 x = sum_part(part, fj + jj, fslot);
      o[jj] = mfma32(u32x4{p1[0], p1[1], 0u, 0u}, u32x4{pack_bf16(x[0], x[1]), pack_bf16(x[2], x[3]), 0u, 0u}, f32x4{0.f, 0.f, 0.f, 0.f});
    }
    const __amdgpu_buffer_rsrc_t ro = out_srd(out, bh0);
    const unsigned oo = SQ * DH * 2 + (16 * u + 4 * g) * (DH * 2) + h * 128 + c * 8;
#pragma unroll
    for (int t = 0; t < 4; ++t)
      __builtin_amdgcn_raw_buffer_store_b64(u32x2{pack_bf16(o[0][t], o[1][t]), pack_bf16(o[2][t], o[3][t])}, ro, oo + t * (DH * 2), 0, AUX_SC1);
  }
}

extern "C" void kernel_launch(void* const* d_in, const int* in_sizes, int n_in, void* d_out, int out_size,
                              void* d_ws, size_t ws_size, hipStream_t stream) {
  const __hip_bfloat16* q = (const __hip_bfloat16*)d_in[0];
  const __hip_bfloat16* k = (const __hip_bfloat16*)d_in[1];
  const __hip_bfloat16* v = (const __hip_bfloat16*)d_in[2];
  const __hip_bfloat16* e = (const __hip_bfloat16*)d_in[3];
  const __hip_bfloat16* f = (const __hip_bfloat16*)d_in[4];
  (void)hipFuncSetAttribute((const void*)linformer_kernel, hipFuncAttributeMaxDynamicSharedMemorySize, LDS_BYTES);
  linformer_kernel<<<NB * NH / HPW, (NW + 1) * 64, LDS_BYTES, stream>>>(q, k, v, e, f, (__hip_bfloat16*)d_out);
}
```

```cpp
#include <hip/hip_runtime.h>
#include <hip/hip_bf16.h>
#include <stdint.h>

typedef unsigned u32x4 __attribute__((ext_vector_type(4)));
typedef unsigned u32x2 __attribute__((ext_vector_type(2)));
typedef float f32x4 __attribute__((ext_vector_type(4)));
typedef __bf16 bf16x8 __attribute__((ext_vector_type(8)));
#define GAS __attribute__((address_space(1)))
#define LAS __attribute__((address_space(3)))

constexpr int NB = 16, NH = 32, SQ = 32, SKV = 4096, DH = 128;
constexpr int NW = 4;
constexpr int HPW = 2;
constexpr int TPS = SKV / 32 / NW;
constexpr int NSEQ = TPS * 2 * HPW;
constexpr int HEAD_BYTES = SKV * DH * 2;
constexpr int AUX_NT = 2, AUX_SC1 = 16;

constexpr int NFLY = 8, WIN_MID = 5, WIN_AHEAD = 4;
constexpr unsigned PERIOD4 = 872;
constexpr unsigned T_START = 250;
constexpr unsigned T_LAG = 75;
constexpr int SPRINT = 2;
constexpr unsigned T_STALL = 180;

__device__ __forceinline__ f32x4 mfma32(u32x4 a, u32x4 b, f32x4 c) {
  return __builtin_amdgcn_mfma_f32_16x16x32_bf16(__builtin_bit_cast(bf16x8, a), __builtin_bit_cast(bf16x8, b), c, 0, 0, 0);
}
__device__ __forceinline__ unsigned pack_bf16(float lo, float hi) {
  typedef __bf16 bf16x2 __attribute__((ext_vector_type(2)));
  bf16x2 v; v.x = (__bf16)lo; v.y = (__bf16)hi;
  return __builtin_bit_cast(unsigned, v);
}
__device__ __forceinline__ float round_bf16(float x) { return (float)(__bf16)x; }

struct Ctx {
  const char* k; const char* v; const char* e; const char* f;
  unsigned xo, eo;
  int tw;
};
struct Srd { __amdgpu_buffer_rsrc_t x, e; };

__device__ __forceinline__ Srd tile_srd(const Ctx& c, int s) {
  const int phase = (s / TPS) & 3, t = ((s % TPS) * NW + c.tw) & (SKV / 32 - 1);
  const bool valid = s < NSEQ;
  const char* xp = ((phase & 2) ? c.v : c.k) + (size_t)(phase & 1) * HEAD_BYTES + (size_t)t * 8192;
  const char* ep = ((phase & 2) ? c.f : c.e) + (size_t)t * 64;
  Srd r;
  r.x = __builtin_amdgcn_make_buffer_rsrc((void*)xp, 0, valid ? 8192 : 0, 0x00020000);
  r.e = __builtin_amdgcn_make_buffer_rsrc((void*)ep, 0, valid ? 15 * SKV * 2 + 64 : 0, 0x00020000);
  return r;
}
__device__ __forceinline__ u32x4 ld_x(const Srd& r, const Ctx& c, int row) {
  return __builtin_amdgcn_raw_buffer_load_b128(r.x, c.xo + row * (DH * 2), 0, AUX_NT);
}
__device__ __forceinline__ u32x4 ld_e(const Srd& r, const Ctx& c) { return __builtin_amdgcn_raw_buffer_load_b128(r.e, c.eo, 0, 0); }

constexpr int PART_W = 8192, PART_BUF = NW * PART_W;
constexpr int LDS_PART = 0, LDS_Q = 4 * PART_BUF, LDS_KB = LDS_Q + 16384, LDS_P1 = LDS_KB + 4096, LDS_CNT = LDS_P1 + 1024;
constexpr int LDS_BYTES = LDS_CNT + 16;

__device__ __forceinline__ f32x4 sum_part(const unsigned char* part, int j, int slot) {
  const unsigned char* p = part + j * 1024 + slot * 16;
  f32x4 x = *(const f32x4*)p;
#pragma unroll
  for (int w = 1; w < NW; ++w) x += *(const f32x4*)(p + w * PART_W);
  return x;
}

__device__ __forceinline__ u32x2 softmax_p(const unsigned char* smem, int hh, int u, int lane) {
  const int c = lane & 15, g = lane >> 4;
  const unsigned char* qp = smem + LDS_Q + hh * 8192 + (16 * u + c) * 256;
  const unsigned char* kp = smem + LDS_KB + c * 256;
  f32x4 s = f32x4{0.f, 0.f, 0.f, 0.f};
#pragma unroll
  for (int st = 0; st < 4; ++st) {
    const int pos = ((4 * st + g) ^ c) << 4;
    s = mfma32(*(const u32x4*)(kp + pos), *(const u32x4*)(qp + pos), s);
  }
  float sc[4];
#pragma unroll
  for (int t = 0; t < 4; ++t) sc[t] = round_bf16(round_bf16(s[t]) * 0.08838834764831845f);
  float mx = fmaxf(fmaxf(sc[0], sc[1]), fmaxf(sc[2], sc[3]));
  mx = fmaxf(mx, __shfl_xor(mx, 16));
  mx = fmaxf(mx, __shfl_xor(mx, 32));
  float sum = 0.f;
#pragma unroll
  for (int t = 0; t < 4; ++t) { sc[t] = __expf(sc[t] - mx); sum += sc[t]; }
  sum += __shfl_xor(sum, 16);
  sum += __shfl_xor(sum, 32);
  const float inv = 1.0f / sum;
  return u32x2{pack_bf16(sc[0] * inv, sc[1] * inv), pack_bf16(sc[2] * inv, sc[3] * inv)};
}

__device__ __forceinline__ void wait_partials(const unsigned char* smem, int phase) {
  volatile LAS unsigned* cnt = (volatile LAS unsigned*)(LAS unsigned char*)(smem + LDS_CNT) + phase;
  while (*cnt < (unsigned)NW) __builtin_amdgcn_s_sleep(16);
  __builtin_amdgcn_fence(__ATOMIC_ACQUIRE, "workgroup");
}

__device__ __forceinline__ __amdgpu_buffer_rsrc_t out_srd(__hip_bfloat16* out, int bh0) {
  return __builtin_amdgcn_make_buffer_rsrc((char*)out + (size_t)bh0 * (SQ * DH * 2), 0, HPW * SQ * DH * 2, 0x00020000);
}

__global__ void __launch_bounds__((NW + 1) * 64, 2)
linformer_kernel(const __hip_bfloat16* __restrict__ q, const __hip_bfloat16* __restrict__ k,
                 const __hip_bfloat16* __restrict__ v, const __hip_bfloat16* __restrict__ e,
                 const __hip_bfloat16* __restrict__ f, __hip_bfloat16* __restrict__ out) {
  extern __shared__ __attribute__((aligned(16))) unsigned char smem[];
  const int tid = threadIdx.x, lane = tid & 63;
  const int wave = __builtin_amdgcn_readfirstlane(tid >> 6);
  const int c = lane & 15, g = lane >> 4;
  const int bh0 = blockIdx.x * HPW;

  if (wave == NW) {
    if (lane < 4) *((LAS unsigned*)(LAS unsigned char*)(smem + LDS_CNT) + lane) = 0u;
    __syncthreads();
    {
      const GAS char* qg = (const GAS char*)q + (size_t)bh0 * (SQ * DH * 2);
      u32x4 qreg[16];
#pragma unroll
      for (int i = 0; i < 16; ++i) qreg[i] = __builtin_nontemporal_load((const GAS u32x4*)(qg + (i * 64 + lane) * 16));
#pragma unroll
      for (int i = 0; i < 16; ++i) {
        const int row = i * 4 + g;
        *(u32x4*)(smem + LDS_Q + row * 256 + ((c ^ (row & 15)) << 4)) = qreg[i];
      }
    }
    u32x2 p0[2];
#pragma unroll 1
    for (int hh = 0; hh < 2; ++hh) {
      wait_partials(smem, hh);
      f32x4 x[8];
#pragma unroll
      for (int j = 0; j < 8; ++j) x[j] = sum_part(smem + LDS_PART + hh * PART_BUF, j, lane ^ ((j >> 2) << 3));
#pragma unroll
      for (int t = 0; t < 4; ++t) {
        const int r = 4 * g + t;
        *(u32x4*)(smem + LDS_KB + r * 256 + ((c ^ r) << 4)) =
            u32x4{pack_bf16(x[0][t], x[1][t]), pack_bf16(x[2][t], x[3][t]), pack_bf16(x[4][t], x[5][t]), pack_bf16(x[6][t], x[7][t])};
      }
      const u32x2 pa = softmax_p(smem, hh, 0, lane), pb = softmax_p(smem, hh, 1, lane);
      if (hh == 0) { p0[0] = pa; p0[1] = pb; }
      else { *(u32x2*)(smem + LDS_P1 + lane * 8) = pa; *(u32x2*)(smem + LDS_P1 + 512 + lane * 8) = pb; }
    }
    wait_partials(smem, 2);
    u32x4 o0[2][4];
    {
      u32x2 b[8];
#pragma unroll
      for (int j = 0; j < 8; ++j) {
        const f32x4 x = sum_part(smem + LDS_PART + 2 * PART_BUF, j, lane ^ ((j >> 2) << 3));
        b[j] = u32x2{pack_bf16(x[0], x[1]), pack_bf16(x[2], x[3])};
      }
#pragma unroll
      for (int u = 0; u < 2; ++u) {
        f32x4 o[8];
#pragma unroll
        for (int j = 0; j < 8; ++j)
          o[j] = mfma32(u32x4{p0[u][0], p0[u][1], 0u, 0u}, u32x4{b[j][0], b[j][1], 0u, 0u}, f32x4{0.f, 0.f, 0.f, 0.f});
#pragma unroll
        for (int t = 0; t < 4; ++t)
          o0[u][t] = u32x4{pack_bf16(o[0][t], o[1][t]), pack_bf16(o[2][t], o[3][t]), pack_bf16(o[4][t], o[5][t]), pack_bf16(o[6][t], o[7][t])};
      }
    }
    __syncthreads();
    const __amdgpu_buffer_rsrc_t ro = out_srd(out, bh0);
#pragma unroll
    for (int u = 0; u < 2; ++u)
#pragma unroll
      for (int t = 0; t < 4; ++t)
        __builtin_amdgcn_raw_buffer_store_b128(o0[u][t], ro, (16 * u + 4 * g + t) * (DH * 2) + c * 16, 0, AUX_SC1);
    return;
  }

  unsigned sched_t0 = (unsigned)__builtin_amdgcn_s_memrealtime(), prev_t = sched_t0 + T_START;
  Ctx cx;
  cx.k = (const char*)k + (size_t)bh0 * HEAD_BYTES;
  cx.v = (const char*)v + (size_t)bh0 * HEAD_BYTES;
  cx.e = (const char*)e;
  cx.f = (const char*)f;
  cx.xo = (unsigned)(g * 8 * (DH * 2) + c * 16);
  cx.eo = (unsigned)(c * (SKV * 2) + g * 16);
  cx.tw = wave + (blockIdx.x >> 3) * NW;

  u32x4 R[16], ef[2];
  Srd cur = tile_srd(cx, 0);
  ef[0] = ld_e(cur, cx);
#pragma unroll
  for (int L = 0; L < NFLY; ++L) R[L] = ld_x(cur, cx, L);
  static_assert(NFLY == 8, "the ring below requests tile s + 1 while it consumes tile s");
  __builtin_amdgcn_sched_barrier(0);
  __syncthreads();

#pragma unroll 1
  for (int phase = 0; phase < 2 * HPW; ++phase) {
    f32x4 acc[8];
#pragma unroll
    for (int j = 0; j < 8; ++j) acc[j] = f32x4{0.f, 0.f, 0.f, 0.f};
#pragma unroll 1
    for (int it = 0; it < TPS / 2; ++it) {
      const int s0 = phase * TPS + it * 2;
      const unsigned now = (unsigned)__builtin_amdgcn_s_memrealtime(), dt = now - prev_t;
      prev_t = now;
      if (s0 > 0 && (int)dt > (int)(PERIOD4 / 4 + T_STALL)) sched_t0 += dt - PERIOD4 / 4;
      const unsigned el = now - sched_t0, tg = (unsigned)(s0 >> 1) * PERIOD4 / 4 + T_START;
      const unsigned lvl = __builtin_amdgcn_readfirstlane((s0 >> 1) >= NSEQ / 2 - SPRINT ? 0u : (el < tg ? 2u : (el < tg + T_LAG ? 1u : 0u)));
      u32x4 lo[4], hi[4];
#pragma unroll
      for (int st = 0; st < 8; ++st) {
        if ((st & 3) == 0) cur = tile_srd(cx, s0 + (st >> 2) + 1);
        __builtin_amdgcn_sched_barrier(0);
#pragma unroll
        for (int w = 0; w < 4; ++w) {
          lo[w][st & 3] = __builtin_amdgcn_perm(R[2 * st + 1][w], R[2 * st][w], 0x05040100u);
          hi[w][st & 3] = __builtin_amdgcn_perm(R[2 * st + 1][w], R[2 * st][w], 0x07060302u);
        }
        __builtin_amdgcn_sched_barrier(0);
        asm volatile("s_cmp_eq_u32 %0, 0\n\ts_cbranch_scc1 .Lthr%=\n\ts_waitcnt vmcnt(%1)\n\ts_cmp_eq_u32 %0, 1\n\t"
                     "s_cbranch_scc1 .Lthr%=\n\ts_waitcnt vmcnt(%2)\n.Lthr%=:" ::"s"(lvl), "n"(WIN_MID), "n"(WIN_AHEAD) : "scc");
        if ((st & 3) == 0) ef[((st >> 2) + 1) & 1] = ld_e(cur, cx);
        R[(2 * st + NFLY) % 16] = ld_x(cur, cx, (2 * st) & 7);
        R[(2 * st + NFLY + 1) % 16] = ld_x(cur, cx, (2 * st + 1) & 7);
        __builtin_amdgcn_sched_barrier(0);
        if ((st & 3) == 3) {
#pragma unroll
          for (int w = 0; w < 4; ++w) {
            acc[2 * w] = mfma32(ef[st >> 2], lo[w], acc[2 * w]);
            acc[2 * w + 1] = mfma32(ef[st >> 2], hi[w], acc[2 * w + 1]);
          }
          __builtin_amdgcn_sched_barrier(0);
        }
      }
    }
    unsigned char* part = smem + LDS_PART + phase * PART_BUF + wave * PART_W;
#pragma unroll
    for (int j = 0; j < 8; ++j) *(f32x4*)(part + j * 1024 + (lane ^ ((j >> 2) << 3)) * 16) = acc[j];
    if (phase < 3) {
      __builtin_amdgcn_fence(__ATOMIC_RELEASE, "workgroup");
      if (lane == 0)
        __hip_atomic_fetch_add((LAS unsigned*)(LAS unsigned char*)(smem + LDS_CNT) + phase, 1u, __ATOMIC_RELAXED, __HIP_MEMORY_SCOPE_WORKGROUP);
    }
  }
  __syncthreads();
  {
    const int u = wave & 1, h = wave >> 1;
    const int fslot = (8 * h + (c >> 1) + 16 * g) ^ ((c & 1) << 3), fj = 4 * (c & 1);
    const unsigned char* part = smem + LDS_PART + 3 * PART_BUF;
    const u32x2 p1 = *(const u32x2*)(smem + LDS_P1 + u * 512 + lane * 8);
    f32x4 o[4];
#pragma unroll
    for (int jj = 0; jj < 4; ++jj) {
      const f32x4 x = sum_part(part, fj + jj, fslot);
      o[jj] = mfma32(u32x4{p1[0], p1[1], 0u, 0u}, u32x4{pack_bf16(x[0], x[1]), pack_bf16(x[2], x[3]), 0u, 0u}, f32x4{0.f, 0.f, 0.f, 0.f});
    }
    const __amdgpu_buffer_rsrc_t ro = out_srd(out, bh0);
    const unsigned oo = SQ * DH * 2 + (16 * u + 4 * g) * (DH * 2) + h * 128 + c * 8;
#pragma unroll
    for (int t = 0; t < 4; ++t)
      __builtin_amdgcn_raw_buffer_store_b64(u32x2{pack_bf16(o[0][t], o[1][t]), pack_bf16(o[2][t], o[3][t])}, ro, oo + t * (DH * 2), 0, AUX_SC1);
  }
}

extern "C" void kernel_launch(void* const* d_in, const int* in_sizes, int n_in, void* d_out, int out_size,
                              void* d_ws, size_t ws_size, hipStream_t stream) {
  const __hip_bfloat16* q = (const __hip_bfloat16*)d_in[0];
  const __hip_bfloat16* k = (const __hip_bfloat16*)d_in[1];
  const __hip_bfloat16* v = (const __hip_bfloat16*)d_in[2];
  const __hip_bfloat16* e = (const __hip_bfloat16*)d_in[3];
  const __hip_bfloat16* f = (const __hip_bfloat16*)d_in[4];
  (void)hipFuncSetAttribute((const void*)linformer_kernel, hipFuncAttributeMaxDynamicSharedMemorySize, LDS_BYTES);
  linformer_kernel<<<NB * NH / HPW, (NW + 1) * 64, LDS_BYTES, stream>>>(q, k, v, e, f, (__hip_bfloat16*)d_out);
}
```

```cpp
#include <hip/hip_runtime.h>
#include <hip/hip_bf16.h>
#include <stdint.h>

typedef unsigned u32x4 __attribute__((ext_vector_type(4)));
typedef unsigned u32x2 __attribute__((ext_vector_type(2)));
typedef float f32x4 __attribute__((ext_vector_type(4)));
typedef __bf16 bf16x8 __attribute__((ext_vector_type(8)));
#define GAS __attribute__((address_space(1)))
#define LAS __attribute__((address_space(3)))

constexpr int NB = 16, NH = 32, SQ = 32, SKV = 4096, DH = 128;
constexpr int NW = 4;
constexpr int HPW = 2;
constexpr int TPS = SKV / 32 / NW;
constexpr int NSEQ = TPS * 2 * HPW;
constexpr int HEAD_BYTES = SKV * DH * 2;
constexpr int AUX_NT = 2, AUX_SC1 = 16;

constexpr int NFLY = 8, WIN_MID = 5, WIN_AHEAD = 4;
constexpr unsigned PERIOD4 = 860;
constexpr unsigned T_START = 400;
constexpr unsigned T_LAG = 75;
constexpr int SPRINT = 2;
constexpr unsigned T_STALL = 180;

__device__ __forceinline__ f32x4 mfma32(u32x4 a, u32x4 b, f32x4 c) {
  return __builtin_amdgcn_mfma_f32_16x16x32_bf16(__builtin_bit_cast(bf16x8, a), __builtin_bit_cast(bf16x8, b), c, 0, 0, 0);
}
__device__ __forceinline__ unsigned pack_bf16(float lo, float hi) {
  typedef __bf16 bf16x2 __attribute__((ext_vector_type(2)));
  bf16x2 v; v.x = (__bf16)lo; v.y = (__bf16)hi;
  return __builtin_bit_cast(unsigned, v);
}
__device__ __forceinline__ float round_bf16(float x) { return (float)(__bf16)x; }

struct Ctx {
  const char* k; const char* v; const char* e; const char* f;
  unsigned xo, eo;
  int tw;
};
struct Srd { __amdgpu_buffer_rsrc_t x, e; };

__device__ __forceinline__ Srd tile_srd(const Ctx& c, int s) {
  const int phase = (s / TPS) & 3, t = ((s % TPS) * NW + c.tw) & (SKV / 32 - 1);
  const bool valid = s < NSEQ;
  const char* xp = ((phase & 2) ? c.v : c.k) + (size_t)(phase & 1) * HEAD_BYTES + (size_t)t * 8192;
  const char* ep = ((phase & 2) ? c.f : c.e) + (size_t)t * 64;
  Srd r;
  r.x = __builtin_amdgcn_make_buffer_rsrc((void*)xp, 0, valid ? 8192 : 0, 0x00020000);
  r.e = __builtin_amdgcn_make_buffer_rsrc((void*)ep, 0, valid ? 15 * SKV * 2 + 64 : 0, 0x00020000);
  return r;
}
__device__ __forceinline__ u32x4 ld_x(const Srd& r, const Ctx& c, int row) {
  return __builtin_amdgcn_raw_buffer_load_b128(r.x, c.xo + row * (DH * 2), 0, AUX_NT);
}
__device__ __forceinline__ u32x4 ld_e(const Srd& r, const Ctx& c) { return __builtin_amdgcn_raw_buffer_load_b128(r.e, c.eo, 0, 0); }

constexpr int PART_W = 8192, PART_BUF = NW * PART_W;
constexpr int LDS_PART = 0, LDS_Q = 4 * PART_BUF, LDS_KB = LDS_Q + 16384, LDS_P1 = LDS_KB + 4096, LDS_CNT = LDS_P1 + 1024;
constexpr int LDS_BYTES = LDS_CNT + 16;

__device__ __forceinline__ f32x4 sum_part(const unsigned char* part, int j, int slot) {
  const unsigned char* p = part + j * 1024 + slot * 16;
  f32x4 x = *(const f32x4*)p;
#pragma unroll
  for (int w = 1; w < NW; ++w) x += *(const f32x4*)(p + w * PART_W);
  return x;
}

__device__ __forceinline__ u32x2 softmax_p(const unsigned char* smem, int hh, int u, int lane) {
  const int c = lane & 15, g = lane >> 4;
  const unsigned char* qp = smem + LDS_Q + hh * 8192 + (16 * u + c) * 256;
  const unsigned char* kp = smem + LDS_KB + c * 256;
  f32x4 s = f32x4{0.f, 0.f, 0.f, 0.f};
#pragma unroll
  for (int st = 0; st < 4; ++st) {
    const int pos = ((4 * st + g) ^ c) << 4;
    s = mfma32(*(const u32x4*)(kp + pos), *(const u32x4*)(qp + pos), s);
  }
  float sc[4];
#pragma unroll
  for (int t = 0; t < 4; ++t) sc[t] = round_bf16(round_bf16(s[t]) * 0.08838834764831845f);
  float mx = fmaxf(fmaxf(sc[0], sc[1]), fmaxf(sc[2], sc[3]));
  mx = fmaxf(mx, __shfl_xor(mx, 16));
  mx = fmaxf(mx, __shfl_xor(mx, 32));
  float sum = 0.f;
#pragma unroll
  for (int t = 0; t < 4; ++t) { sc[t] = __expf(sc[t] - mx); sum += sc[t]; }
  sum += __shfl_xor(sum, 16);
  sum += __shfl_xor(sum, 32);
  const float inv = 1.0f / sum;
  return u32x2{pack_bf16(sc[0] * inv, sc[1] * inv), pack_bf16(sc[2] * inv, sc[3] * inv)};
}

__device__ __forceinline__ void wait_partials(const unsigned char* smem, int phase) {
  volatile LAS unsigned* cnt = (volatile LAS unsigned*)(LAS unsigned char*)(smem + LDS_CNT) + phase;
  while (*cnt < (unsigned)NW) __builtin_amdgcn_s_sleep(16);
  __builtin_amdgcn_fence(__ATOMIC_ACQUIRE, "workgroup");
}

__device__ __forceinline__ __amdgpu_buffer_rsrc_t out_srd(__hip_bfloat16* out, int bh0) {
  return __builtin_amdgcn_make_buffer_rsrc((char*)out + (size_t)bh0 * (SQ * DH * 2), 0, HPW * SQ * DH * 2, 0x00020000);
}

__global__ void __launch_bounds__((NW + 1) * 64, 2)
linformer_kernel(const __hip_bfloat16* __restrict__ q, const __hip_bfloat16* __restrict__ k,
                 const __hip_bfloat16* __restrict__ v, const __hip_bfloat16* __restrict__ e,
                 const __hip_bfloat16* __restrict__ f, __hip_bfloat16* __restrict__ out) {
  extern __shared__ __attribute__((aligned(16))) unsigned char smem[];
  const int tid = threadIdx.x, lane = tid & 63;
  const int wave = __builtin_amdgcn_readfirstlane(tid >> 6);
  const int c = lane & 15, g = lane >> 4;
  const int bh0 = blockIdx.x * HPW;

  if (wave == NW) {
    if (lane < 4) *((LAS unsigned*)(LAS unsigned char*)(smem + LDS_CNT) + lane) = 0u;
    __syncthreads();
    {
      const GAS char* qg = (const GAS char*)q + (size_t)bh0 * (SQ * DH * 2);
      u32x4 qreg[16];
#pragma unroll
      for (int i = 0; i < 16; ++i) qreg[i] = __builtin_nontemporal_load((const GAS u32x4*)(qg + (i * 64 + lane) * 16));
#pragma unroll
      for (int i = 0; i < 16; ++i) {
        const int row = i * 4 + g;
        *(u32x4*)(smem + LDS_Q + row * 256 + ((c ^ (row & 15)) << 4)) = qreg[i];
      }
    }
    u32x2 p0[2];
#pragma unroll 1
    for (int hh = 0; hh < 2; ++hh) {
      wait_partials(smem, hh);
      f32x4 x[8];
#pragma unroll
      for (int j = 0; j < 8; ++j) x[j] = sum_part(smem + LDS_PART + hh * PART_BUF, j, lane ^ ((j >> 2) << 3));
#pragma unroll
      for (int t = 0; t < 4; ++t) {
        const int r = 4 * g + t;
        *(u32x4*)(smem + LDS_KB + r * 256 + ((c ^ r) << 4)) =
            u32x4{pack_bf16(x[0][t], x[1][t]), pack_bf16(x[2][t], x[3][t]), pack_bf16(x[4][t], x[5][t]), pack_bf16(x[6][t], x[7][t])};
      }
      const u32x2 pa = softmax_p(smem, hh, 0, lane), pb = softmax_p(smem, hh, 1, lane);
      if (hh == 0) { p0[0] = pa; p0[1] = pb; }
      else { *(u32x2*)(smem + LDS_P1 + lane * 8) = pa; *(u32x2*)(smem + LDS_P1 + 512 + lane * 8) = pb; }
    }
    wait_partials(smem, 2);
    u32x4 o0[2][4];
    {
      u32x2 b[8];
#pragma unroll
      for (int j = 0; j < 8; ++j) {
        const f32x4 x = sum_part(smem + LDS_PART + 2 * PART_BUF, j, lane ^ ((j >> 2) << 3));
        b[j] = u32x2{pack_bf16(x[0], x[1]), pack_bf16(x[2], x[3])};
      }
#pragma unroll
      for (int u = 0; u < 2; ++u) {
        f32x4 o[8];
#pragma unroll
        for (int j = 0; j < 8; ++j)
          o[j] = mfma32(u32x4{p0[u][0], p0[u][1], 0u, 0u}, u32x4{b[j][0], b[j][1], 0u, 0u}, f32x4{0.f, 0.f, 0.f, 0.f});
#pragma unroll
        for (int t = 0; t < 4; ++t)
          o0[u][t] = u32x4{pack_bf16(o[0][t], o[1][t]), pack_bf16(o[2][t], o[3][t]), pack_bf16(o[4][t], o[5][t]), pack_bf16(o[6][t], o[7][t])};
      }
    }
    __syncthreads();
    const __amdgpu_buffer_rsrc_t ro = out_srd(out, bh0);
#pragma unroll
    for (int u = 0; u < 2; ++u)
#pragma unroll
      for (int t = 0; t < 4; ++t)
        __builtin_amdgcn_raw_buffer_store_b128(o0[u][t], ro, (16 * u + 4 * g + t) * (DH * 2) + c * 16, 0, AUX_SC1);
    return;
  }

  unsigned sched_t0 = (unsigned)__builtin_amdgcn_s_memrealtime(), prev_t = sched_t0;
  Ctx cx;
  cx.k = (const char*)k + (size_t)bh0 * HEAD_BYTES;
  cx.v = (const char*)v + (size_t)bh0 * HEAD_BYTES;
  cx.e = (const char*)e;
  cx.f = (const char*)f;
  cx.xo = (unsigned)(g * 8 * (DH * 2) + c * 16);
  cx.eo = (unsigned)(c * (SKV * 2) + g * 16);
  cx.tw = wave + (blockIdx.x >> 3) * NW;

  u32x4 R[16], ef[2];
  Srd cur = tile_srd(cx, 0);
  ef[0] = ld_e(cur, cx);
#pragma unroll
  for (int L = 0; L < NFLY; ++L) R[L] = ld_x(cur, cx, L);
  static_assert(NFLY == 8, "the ring below requests tile s + 1 while it consumes tile s");
  __builtin_amdgcn_sched_barrier(0);
  __syncthreads();

#pragma unroll 1
  for (int phase = 0; phase < 2 * HPW; ++phase) {
    f32x4 acc[8];
#pragma unroll
    for (int j = 0; j < 8; ++j) acc[j] = f32x4{0.f, 0.f, 0.f, 0.f};
#pragma unroll 1
    for (int it = 0; it < TPS / 2; ++it) {
      const int s0 = phase * TPS + it * 2;
      const unsigned now = (unsigned)__builtin_amdgcn_s_memrealtime(), dt = now - prev_t;
      prev_t = now;
      if (s0 > 2 && dt > PERIOD4 / 4 + T_STALL) sched_t0 += dt - PERIOD4 / 4;
      const unsigned el = now - sched_t0, tg = (unsigned)(s0 >> 1) * PERIOD4 / 4 + T_START;
      const unsigned lvl = __builtin_amdgcn_readfirstlane((s0 >> 1) >= NSEQ / 2 - SPRINT ? 0u : (el < tg ? 2u : (el < tg + T_LAG ? 1u : 0u)));
      u32x4 lo[4], hi[4];
#pragma unroll
      for (int st = 0; st < 8; ++st) {
        if ((st & 3) == 0) cur = tile_srd(cx, s0 + (st >> 2) + 1);
        __builtin_amdgcn_sched_barrier(0);
#pragma unroll
        for (int w = 0; w < 4; ++w) {
          lo[w][st & 3] = __builtin_amdgcn_perm(R[2 * st + 1][w], R[2 * st][w], 0x05040100u);
          hi[w][st & 3] = __builtin_amdgcn_perm(R[2 * st + 1][w], R[2 * st][w], 0x07060302u);
        }
        __builtin_amdgcn_sched_barrier(0);
        asm volatile("s_cmp_eq_u32 %0, 0\n\ts_cbranch_scc1 .Lthr%=\n\ts_waitcnt vmcnt(%1)\n\ts_cmp_eq_u32 %0, 1\n\t"
                     "s_cbranch_scc1 .Lthr%=\n\ts_waitcnt vmcnt(%2)\n.Lthr%=:" ::"s"(lvl), "n"(WIN_MID), "n"(WIN_AHEAD) : "scc");
        if ((st & 3) == 0) ef[((st >> 2) + 1) & 1] = ld_e(cur, cx);
        R[(2 * st + NFLY) % 16] = ld_x(cur, cx, (2 * st) & 7);
        R[(2 * st + NFLY + 1) % 16] = ld_x(cur, cx, (2 * st + 1) & 7);
        __builtin_amdgcn_sched_barrier(0);
        if ((st & 3) == 3) {
#pragma unroll
          for (int w = 0; w < 4; ++w) {
            acc[2 * w] = mfma32(ef[st >> 2], lo[w], acc[2 * w]);
            acc[2 * w + 1] = mfma32(ef[st >> 2], hi[w], acc[2 * w + 1]);
          }
          __builtin_amdgcn_sched_barrier(0);
        }
      }
    }
    unsigned char* part = smem + LDS_PART + phase * PART_BUF + wave * PART_W;
#pragma unroll
    for (int j = 0; j < 8; ++j) *(f32x4*)(part + j * 1024 + (lane ^ ((j >> 2) << 3)) * 16) = acc[j];
    if (phase < 3) {
      __builtin_amdgcn_fence(__ATOMIC_RELEASE, "workgroup");
      if (lane == 0)
        __hip_atomic_fetch_add((LAS unsigned*)(LAS unsigned char*)(smem + LDS_CNT) + phase, 1u, __ATOMIC_RELAXED, __HIP_MEMORY_SCOPE_WORKGROUP);
    }
  }
  __syncthreads();
  {
    const int u = wave & 1, h = wave >> 1;
    const int fslot = (8 * h + (c >> 1) + 16 * g) ^ ((c & 1) << 3), fj = 4 * (c & 1);
    const unsigned char* part = smem + LDS_PART + 3 * PART_BUF;
    const u32x2 p1 = *(const u32x2*)(smem + LDS_P1 + u * 512 + lane * 8);
    f32x4 o[4];
#pragma unroll
    for (int jj = 0; jj < 4; ++jj) {
      const f32x4 x = sum_part(part, fj + jj, fslot);
      o[jj] = mfma32(u32x4{p1[0], p1[1], 0u, 0u}, u32x4{pack_bf16(x[0], x[1]), pack_bf16(x[2], x[3]), 0u, 0u}, f32x4{0.f, 0.f, 0.f, 0.f});
    }
    const __amdgpu_buffer_rsrc_t ro = out_srd(out, bh0);
    const unsigned oo = SQ * DH * 2 + (16 * u + 4 * g) * (DH * 2) + h * 128 + c * 8;
#pragma unroll
    for (int t = 0; t < 4; ++t)
      __builtin_amdgcn_raw_buffer_store_b64(u32x2{pack_bf16(o[0][t], o[1][t]), pack_bf16(o[2][t], o[3][t])}, ro, oo + t * (DH * 2), 0, AUX_SC1);
  }
}

extern "C" void kernel_launch(void* const* d_in, const int* in_sizes, int n_in, void* d_out, int out_size,
                              void* d_ws, size_t ws_size, hipStream_t stream) {
  const __hip_bfloat16* q = (const __hip_bfloat16*)d_in[0];
  const __hip_bfloat16* k = (const __hip_bfloat16*)d_in[1];
  const __hip_bfloat16* v = (const __hip_bfloat16*)d_in[2];
  const __hip_bfloat16* e = (const __hip_bfloat16*)d_in[3];
  const __hip_bfloat16* f = (const __hip_bfloat16*)d_in[4];
  (void)hipFuncSetAttribute((const void*)linformer_kernel, hipFuncAttributeMaxDynamicSharedMemorySize, LDS_BYTES);
  linformer_kernel<<<NB * NH / HPW, (NW + 1) * 64, LDS_BYTES, stream>>>(q, k, v, e, f, (__hip_bfloat16*)d_out);
}
```
